```python
import torch

def _softmax(x, axis=-1):
    return torch.softmax(x, dim=axis)
def _gelu(x):
    return torch.nn.functional.gelu(x, approximate="tanh")
def _silu(x):
    return torch.nn.functional.silu(x)
def _sigmoid(x):
    return torch.sigmoid(x)
def _layernorm(x, axis=-1, eps=1e-5):
    m = x.mean(dim=axis, keepdim=True)
    v = x.var(dim=axis, keepdim=True, unbiased=False)
    return (x - m) / torch.sqrt(v + eps)
def _scatter_add(tgt, idx, src, axis=0):
    return tgt.clone().index_add_(axis, idx, src)
def _segment_sum(src, seg, n):
    out = torch.zeros((n,) + src.shape[1:], dtype=src.dtype)
    return out.index_add_(0, seg, src)
def _pad(x, pw):
    flat = tuple(v for pair in reversed(pw) for v in pair)
    return torch.nn.functional.pad(x, flat)

kernel_name = 'recurrent_scan__mha_block_cached__swiglu_ffn__decode'

def setup_inputs():

    _w = (lambda a: (lambda t: t.to(torch.bfloat16) if t.is_floating_point() else t)(torch.as_tensor(a)))
    def _A_setup():
        B, Sq, D = 2, 1, 512
        H, Sk, Dh = 32, 16384, 128
        return {
            "x": _w((lambda *s: torch.randn(*s, dtype=torch.bfloat16) * 0.05)(B, Sq, D)),
            "k_cache": _w((lambda *s: torch.randn(*s, dtype=torch.bfloat16) * 1.6)(B, H, Sk, Dh)),
            "v_cache": _w((lambda *s: torch.randn(*s, dtype=torch.bfloat16) * 0.2)(B, H, Sk, Dh)),
            "wq": _w((lambda *s: torch.randn(*s, dtype=torch.bfloat16) * 1.6)(D, H * Dh) * (D ** -0.5)),
            "wo": _w((lambda *s: torch.randn(*s, dtype=torch.bfloat16) * 0.2)(H * Dh, D) * ((H * Dh) ** -0.5)),
        }
    _A_in = _A_setup()

    def _B_setup():
        B, S, D, F = 2, 1, 512, 1792
        return {
            "x": _w((lambda *s: torch.randn(*s, dtype=torch.bfloat16) * 0.05)(B, S, D)),
            "w_gate": _w((lambda *s: torch.randn(*s, dtype=torch.bfloat16) * 0.1)(D, F)),
            "w_up": _w((lambda *s: torch.randn(*s, dtype=torch.bfloat16) * 0.1)(D, F)),
            "w_down": _w((lambda *s: torch.randn(*s, dtype=torch.bfloat16) * 0.1)(F, D)),
        }
    _B_in = _B_setup()

    return {
        "A_x": _A_in["x"],
        "A_k_cache": _A_in["k_cache"],
        "A_v_cache": _A_in["v_cache"],
        "A_wq": _A_in["wq"],
        "A_wo": _A_in["wo"],
        "B_x": _B_in["x"],
        "B_w_gate": _B_in["w_gate"],
        "B_w_up": _B_in["w_up"],
        "B_w_down": _B_in["w_down"],
    }

def reference(A_x, A_k_cache, A_v_cache, A_wq, A_wo, B_x, B_w_gate, B_w_up, B_w_down):
    def _A_ref(x, k_cache, v_cache, wq, wo):
        B, Sq, D = x.shape
        H, Dh = 32, 128
        q = (lambda x, *d: x.permute(*d))((x @ wq).reshape(B, Sq, H, Dh), 0, 2, 1, 3)
        scores = torch.einsum("bhqd,bhkd->bhqk", q, k_cache) * (Dh ** -0.5)
        p = _softmax(scores, axis=-1)
        o = torch.einsum("bhqk,bhkd->bhqd", p, v_cache)
        return (lambda x, *d: x.permute(*d))(o, 0, 2, 1, 3).reshape(B, Sq, H * Dh) @ wo

    def _B_ref(x, w_gate, w_up, w_down):
        g = _silu(x @ w_gate)
        u = x @ w_up
        return (g * u) @ w_down

    x = A_x
    xn = _layernorm(x, axis=-1)
    s = _A_ref(xn, A_k_cache, A_v_cache, A_wq, A_wo)
    g = _silu(_B_ref(xn, B_w_gate, B_w_up, B_w_down))
    return x + s * g
```

```cpp
#include <hip/hip_runtime.h>
#include <hip/hip_bf16.h>
#include <stdint.h>

#define GAS __attribute__((address_space(1)))
#define LAS __attribute__((address_space(3)))
typedef unsigned v4u __attribute__((ext_vector_type(4)));
typedef unsigned v2u __attribute__((ext_vector_type(2)));
typedef float f32x4 __attribute__((ext_vector_type(4)));
typedef float f32x2 __attribute__((ext_vector_type(2)));
typedef __bf16 bf16x2 __attribute__((ext_vector_type(2)));
typedef GAS unsigned long long gu64;
typedef unsigned long long u64;
#define RLX_AGENT __ATOMIC_RELAXED, __HIP_MEMORY_SCOPE_AGENT

namespace {
constexpr int NB = 2, DM = 512, NH = 32, SK = 16384, DH = 128, FF = 1792;
constexpr int NWG = 256, NSW = 8, NTHR = 64 * (NSW + 1);
constexpr int NSPLIT = 4, KEYS_WG = SK / NSPLIT, TILE = 64, NT = KEYS_WG / (TILE * NSW);
constexpr int NCHUNK = FF / 8;
constexpr unsigned SENT = 0xFFFFFFFFu;
constexpr int AUX_SC1 = 16, RSRC_W3 = 0x00020000;
constexpr unsigned SPIN_CAP = 1u << 18;
constexpr int WS_EPOCH = 0, WS_FLAG = 4096, WS_ML = 8192, WS_Y = 16384, WS_Y_BYTES = NB * 128 * 128 * 16;
constexpr int WS_F = WS_Y + WS_Y_BYTES, WS_F_BYTES = NB * 128 * NCHUNK * 16, WS_END = WS_F + WS_F_BYTES;
constexpr int L_PART = 0;
constexpr int L_ML = 4096;
constexpr int L_SCALE = 4096 + 64;
constexpr int L_Y = 4096 + 128;
constexpr int L_BYTES = L_Y + 8 * 512 * 4;
constexpr float LOG2E = 1.4426950408889634f;
constexpr float QK_SCALE = 0.08838834764831845f;

struct Params {
  const unsigned short* x;
  const unsigned short* k;
  const unsigned short* v;
  const unsigned short* wq;
  const unsigned short* wo;
  const unsigned short* wg;
  const unsigned short* wu;
  const unsigned short* wd;
  unsigned short* out;
  unsigned char* ws;
};

__device__ __forceinline__ float u2f(unsigned u) { return __uint_as_float(u); }
__device__ __forceinline__ unsigned f2u(float f) { return __float_as_uint(f); }
__device__ __forceinline__ float dot2(unsigned a, unsigned b, float c) {
  return __builtin_amdgcn_fdot2_f32_bf16(__builtin_bit_cast(bf16x2, a), __builtin_bit_cast(bf16x2, b), c, false);
}
template <int CTRL> __device__ __forceinline__ float dppf(float x) {
  return __builtin_bit_cast(float, __builtin_amdgcn_mov_dpp(__builtin_bit_cast(int, x), CTRL, 0xf, 0xf, true));
}
template <int CTRL> __device__ __forceinline__ unsigned dppu(unsigned x) {
  return (unsigned)__builtin_amdgcn_mov_dpp((int)x, CTRL, 0xf, 0xf, true);
}
constexpr int XOR1 = 0xB1, XOR2 = 0x4E, HMIR = 0x141, MIR = 0x140, ROR8 = 0x128, BCAST = 0x150;
__device__ __forceinline__ float bf2f(unsigned bits16) { return u2f(bits16 << 16); }
__device__ __forceinline__ unsigned f2bf(float f) { return (unsigned)__builtin_bit_cast(unsigned short, (__bf16)f); }
__device__ __forceinline__ float rbf(float f) { return bf2f(f2bf(f)); }
__device__ __forceinline__ unsigned pack2(float lo, float hi) {
  bf16x2 r = {(__bf16)lo, (__bf16)hi};
  return __builtin_bit_cast(unsigned, r);
}
__device__ __forceinline__ float swap32_add(float a, float b) {
  auto s = __builtin_amdgcn_permlane32_swap(f2u(a), f2u(b), false, false);
  return u2f(s[0]) + u2f(s[1]);
}
__device__ __forceinline__ float swap16_add(float a, float b) {
  auto s = __builtin_amdgcn_permlane16_swap(f2u(a), f2u(b), false, false);
  return u2f(s[0]) + u2f(s[1]);
}
__device__ __forceinline__ float swap32_max(float a) {
  auto s = __builtin_amdgcn_permlane32_swap(f2u(a), f2u(a), false, false);
  return fmaxf(u2f(s[0]), u2f(s[1]));
}
__device__ __forceinline__ float swap16_max(float a) {
  auto s = __builtin_amdgcn_permlane16_swap(f2u(a), f2u(a), false, false);
  return fmaxf(u2f(s[0]), u2f(s[1]));
}
__device__ __forceinline__ float wave_sum(float x) {
  x += dppf<XOR1>(x); x += dppf<XOR2>(x); x += dppf<HMIR>(x); x += dppf<MIR>(x);
  x = swap16_add(x, x); return swap32_add(x, x);
}
__device__ __forceinline__ float wave_max(float x) {
  x = fmaxf(x, dppf<XOR1>(x)); x = fmaxf(x, dppf<XOR2>(x)); x = fmaxf(x, dppf<HMIR>(x)); x = fmaxf(x, dppf<MIR>(x));
  x = swap16_max(x); return swap32_max(x);
}
__device__ __forceinline__ float silu(float g) { return g / (1.0f + __expf(-g)); }
__device__ __forceinline__ unsigned clean(float v) {
  const unsigned u = f2u(v);
  return u == SENT ? 0x7FC00000u : u;
}
#define KV_AUX 2
typedef __amdgpu_buffer_rsrc_t rsrc_t;
__device__ __forceinline__ rsrc_t mk_rsrc(const void* base, unsigned bytes) { return __builtin_amdgcn_make_buffer_rsrc((void*)base, 0, bytes, RSRC_W3); }
template <int AUX> __device__ __forceinline__ v4u ld_piece(rsrc_t rs, unsigned voff, unsigned tile_off, int i) {
  return __builtin_amdgcn_raw_buffer_load_b128(rs, voff + (i & 3) * 1024, tile_off + (i >> 2) * 4096, AUX);
}
__device__ __forceinline__ int fresh_lane() {
  int l;
  asm volatile("v_mbcnt_lo_u32_b32 %0, -1, 0\n\tv_mbcnt_hi_u32_b32 %0, -1, %0" : "=v"(l));
  return l;
}
#define SB() __builtin_amdgcn_sched_barrier(0)
#define PIN8(a) asm volatile("" : "+v"(a[0]), "+v"(a[1]), "+v"(a[2]), "+v"(a[3]), "+v"(a[4]), "+v"(a[5]), "+v"(a[6]), "+v"(a[7]))
#define LDS_BARRIER() do { asm volatile("s_waitcnt lgkmcnt(0)" ::: "memory"); __builtin_amdgcn_s_barrier(); asm volatile("" ::: "memory"); } while (0)

__device__ __forceinline__ void ln_stats(v4u xv, float& mean_bf, float& den_bf) {
  float e[8];
#pragma unroll
  for (int j = 0; j < 4; ++j) { e[2 * j] = bf2f(xv[j] & 0xFFFFu); e[2 * j + 1] = u2f(xv[j] & 0xFFFF0000u); }
  float s = 0.f;
#pragma unroll
  for (int j = 0; j < 8; ++j) s += e[j];
  const float mean = wave_sum(s) * (1.0f / DM);
  float q = 0.f;
#pragma unroll
  for (int j = 0; j < 8; ++j) { const float d = e[j] - mean; q = fmaf(d, d, q); }
  const float var = wave_sum(q) * (1.0f / DM);
  mean_bf = rbf(mean);
  den_bf = rbf(sqrtf(rbf(rbf(var) + 1e-5f)));
}
__device__ __forceinline__ float ln_apply(float x, float mean_bf, float den_bf) { return rbf(rbf(x - mean_bf) / den_bf); }

__device__ __forceinline__ f32x2 rows_reduce8(const float (&a)[8]) {
  float t[4];
#pragma unroll
  for (int j = 0; j < 4; ++j) t[j] = swap32_add(a[j], a[j + 4]);
  f32x2 r;
  r[0] = swap16_add(t[0], t[2]);
  r[1] = swap16_add(t[1], t[3]);
  return r;
}

template <int I> __device__ __forceinline__ void pv_step(const v4u& cur, unsigned plo, unsigned phi, float (&acc)[8]) {
  const unsigned bl = dppu<BCAST + I>(plo), bh = dppu<BCAST + I>(phi);
#pragma unroll
  for (int j = 0; j < 4; ++j) {
    acc[2 * j] = dot2(cur[j], bl, acc[2 * j]);
    acc[2 * j + 1] = dot2(cur[j], bh, acc[2 * j + 1]);
  }
}
template <int I, int N> struct Unroll {
  template <class Fn> static __device__ __forceinline__ void run(Fn&& f) {
    f(std::integral_constant<int, I>{});
    Unroll<I + 1, N>::run(f);
  }
};
template <int N> struct Unroll<N, N> {
  template <class Fn> static __device__ __forceinline__ void run(Fn&&) {}
};

__global__ void __launch_bounds__(NTHR) fused_decode_kernel(Params p) {
  __shared__ __attribute__((aligned(16))) unsigned char smem[L_BYTES];
  LAS unsigned char* L = (LAS unsigned char*)smem;
  const int tid = threadIdx.x, lane = tid & 63, w = __builtin_amdgcn_readfirstlane(tid >> 6);
  const int bid = blockIdx.x, xcd = bid & 7, slot = bid >> 3;
  const int h = xcd * 4 + (slot >> 3), b = (slot >> 2) & 1, split = slot & 3;
  const int prod = h * NSPLIT + split;
  const __amdgpu_buffer_rsrc_t rs = __builtin_amdgcn_make_buffer_rsrc((void*)p.ws, 0, WS_END, RSRC_W3);

  if (w < NSW) {
    const int r = lane >> 4, c = lane & 15;
    const GAS v4u* xrow = (const GAS v4u*)(p.x + b * DM);
    const v4u xv = xrow[lane];
    const unsigned xown = ((const GAS unsigned short*)p.x)[b * DM + 64 * w + lane];
    v4u wt[16];
    {
      const rsrc_t rq = mk_rsrc(p.wq + (size_t)(64 * w) * (NH * DH) + h * DH, 64 * NH * DH * 2);
      const unsigned vo = (16 * r) * (NH * DH * 2) + 16 * c;
#pragma unroll
      for (int i = 0; i < 16; ++i) wt[i] = __builtin_amdgcn_raw_buffer_load_b128(rq, vo, i * (NH * DH * 2), 0);
    }
    SB();
    const size_t kvbase = ((size_t)(b * NH + h) * SK + (size_t)split * KEYS_WG + (size_t)w * TILE) * DH;
    constexpr unsigned TSTEP = NSW * TILE * DH * 2;
    const rsrc_t rk = mk_rsrc(p.k + kvbase, (NT - 1) * TSTEP + TILE * DH * 2), rv = mk_rsrc(p.v + kvbase, (NT - 1) * TSTEP + TILE * DH * 2);
    const unsigned voff = 16 * lane;
    v4u ring[16];
#pragma unroll
    for (int i = 0; i < 16; ++i) ring[i] = ld_piece<KV_AUX>(rk, voff, 0, i);
    SB();
    float mean_bf, den_bf;
    ln_stats(xv, mean_bf, den_bf);
    {
      const unsigned xn = f2bf(ln_apply(bf2f(xown), mean_bf, den_bf));
      const unsigned xlo = xn, xhi = xn << 16;
      float acc[8];
#pragma unroll
      for (int j = 0; j < 8; ++j) acc[j] = 0.f;
      Unroll<0, 16>::run([&](auto I) { pv_step<I.value>(wt[I.value], xlo, xhi, acc); });
      const f32x2 qp = rows_reduce8(acc);
      *(LAS f32x2*)(L + L_PART + (w * 128 + 8 * c + 2 * r) * 4) = qp;
    }
    LDS_BARRIER();
    unsigned qpk[4];
    {
      f32x4 a0 = {0.f, 0.f, 0.f, 0.f}, a1 = {0.f, 0.f, 0.f, 0.f};
#pragma unroll
      for (int ww = 0; ww < NSW; ++ww) {
        a0 += *(LAS f32x4*)(L + L_PART + (ww * 128 + 8 * c) * 4);
        a1 += *(LAS f32x4*)(L + L_PART + (ww * 128 + 8 * c + 4) * 4);
      }
      qpk[0] = pack2(a0[0], a0[1]); qpk[1] = pack2(a0[2], a0[3]); qpk[2] = pack2(a1[0], a1[1]); qpk[3] = pack2(a1[2], a1[3]);
    }
    float sc[NT];
#pragma unroll
    for (int t = 0; t < NT; ++t) {
      float x[16];
#pragma unroll
      for (int i = 0; i < 16; ++i) {
        const v4u cur = ring[i];
        ring[i] = (t + 1 < NT) ? ld_piece<KV_AUX>(rk, voff, (t + 1) * TSTEP, i) : ld_piece<KV_AUX>(rv, voff, 0, i);
        float d = dot2(cur[0], qpk[0], 0.f);
        d = dot2(cur[1], qpk[1], d); d = dot2(cur[2], qpk[2], d); d = dot2(cur[3], qpk[3], d);
        asm volatile("" : "+v"(d));
        x[i] = d;
        SB();
      }
#define TR_STEP(HALF, CTRL, BIT) _Pragma("unroll") for (int hh = 0; hh < (HALF); ++hh) { \
        const float lo_ = x[hh], hi_ = x[hh + (HALF)]; const float t1_ = lo_ + dppf<CTRL>(lo_), t2_ = hi_ + dppf<CTRL>(hi_); \
        x[hh] = (c & (BIT)) ? t2_ : t1_; }
      TR_STEP(8, ROR8, 8) TR_STEP(4, HMIR, 4) TR_STEP(2, XOR2, 2) TR_STEP(1, XOR1, 1)
#undef TR_STEP
      sc[t] = rbf(rbf(x[0]) * QK_SCALE);
    }
    float mw = sc[0];
#pragma unroll
    for (int t = 1; t < NT; ++t) mw = fmaxf(mw, sc[t]);
    mw = wave_max(mw);
    unsigned pb[NT];
    {
      float ls = 0.f;
#pragma unroll
      for (int t = 0; t < NT; ++t) {
        const float e = __builtin_amdgcn_exp2f((sc[t] - mw) * LOG2E);
        ls += e;
        pb[t] = f2bf(e);
      }
      ls = wave_sum(ls);
      if (fresh_lane() == 0) { f32x2 ml = {mw, ls}; *(volatile LAS f32x2*)(L + L_ML + w * 8) = ml; }
    }
    float acc[8];
#pragma unroll
    for (int j = 0; j < 8; ++j) acc[j] = 0.f;
    v4u wo[16];
#pragma unroll
    for (int t = 0; t < NT; ++t) {
      if (t == NT - 2) {
        const rsrc_t ro = mk_rsrc(p.wo + (size_t)(h * DH + 16 * w) * DM, 16 * DM * 2);
#pragma unroll
        for (int i = 0; i < 16; ++i) wo[i] = ld_piece<0>(ro, voff, 0, i);
        SB();
      }
      const unsigned plo = pb[t], phi = pb[t] << 16;
      Unroll<0, 16>::run([&](auto I) {
        const v4u cur = ring[I.value];
        if (t + 1 < NT) ring[I.value] = ld_piece<KV_AUX>(rv, voff, (t + 1) * TSTEP, I.value);
        pv_step<I.value>(cur, plo, phi, acc);
        PIN8(acc);
        SB();
      });
      if (t == NT - 2) {
#pragma unroll
        for (int a = 0; a < 8; ++a)
#pragma unroll
          for (int j = 0; j < 4; ++j) {
            const unsigned e0 = wo[2 * a][j], e1 = wo[2 * a + 1][j];
            wo[2 * a][j] = __builtin_amdgcn_perm(e1, e0, 0x05040100u);
            wo[2 * a + 1][j] = __builtin_amdgcn_perm(e1, e0, 0x07060302u);
          }
        SB();
      }
    }
    const int ln = fresh_lane(), r2 = ln >> 4, c2 = ln & 15;
    {
      const f32x2 op = rows_reduce8(acc);
      *(LAS f32x2*)(L + L_PART + (w * 128 + 8 * c2 + 2 * r2) * 4) = op;
    }
    LDS_BARRIER();
    float y[8];
    {
      const int ws_ = ln >> 3, jj = ln & 7;
      f32x2 o2 = *(LAS f32x2*)(L + L_PART + (ws_ * 128 + 16 * w + 2 * jj) * 4);
      const float s_ = *(LAS float*)(L + L_SCALE + ws_ * 4);
      float o0 = o2[0] * s_, o1 = o2[1] * s_;
      o0 += dppf<ROR8>(o0); o1 += dppf<ROR8>(o1);
      o0 = swap16_add(o0, o0); o1 = swap16_add(o1, o1);
      o0 = swap32_add(o0, o0); o1 = swap32_add(o1, o1);
      const unsigned opk = pack2(o0, o1);
#pragma unroll
      for (int j = 0; j < 8; ++j) y[j] = 0.f;
#pragma unroll
      for (int a = 0; a < 8; ++a) {
        const unsigned oa = __builtin_amdgcn_readlane(opk, a);
#pragma unroll
        for (int j = 0; j < 4; ++j) {
          y[2 * j] = dot2(wo[2 * a][j], oa, y[2 * j]);
          y[2 * j + 1] = dot2(wo[2 * a + 1][j], oa, y[2 * j + 1]);
        }
      }
      *(LAS f32x4*)(L + L_Y + (w * 512 + 8 * ln) * 4) = f32x4{y[0], y[1], y[2], y[3]};
      *(LAS f32x4*)(L + L_Y + (w * 512 + 8 * ln + 4) * 4) = f32x4{y[4], y[5], y[6], y[7]};
    }
    LDS_BARRIER();
    {
      f32x4 a0 = *(LAS f32x4*)(L + L_Y + ((2 * r2) * 512 + 64 * w + 4 * c2) * 4);
      a0 += *(LAS f32x4*)(L + L_Y + ((2 * r2 + 1) * 512 + 64 * w + 4 * c2) * 4);
      v4u o;
#pragma unroll
      for (int j = 0; j < 4; ++j) { float t_ = swap16_add(a0[j], a0[j]); t_ = swap32_add(t_, t_); o[j] = clean(t_); }
      if (ln < 16) __builtin_amdgcn_raw_buffer_store_b128(o, rs, WS_Y + ((b * 128 + 16 * w + c2) * 128 + prod) * 16, 0, AUX_SC1);
    }
    return;
  }

  gu64* const epoch = (gu64*)(p.ws + WS_EPOCH);
  gu64* const flags = (gu64*)(p.ws + WS_FLAG);
  gu64* const mlg = (gu64*)(p.ws + WS_ML);
  const u64 tag = __hip_atomic_load(epoch, RLX_AGENT) + 1ull;
  {
    const v4u sent = {SENT, SENT, SENT, SENT};
    __builtin_amdgcn_raw_buffer_store_b128(sent, rs, WS_Y + ((b * 128 + lane) * 128 + prod) * 16, 0, AUX_SC1);
    __builtin_amdgcn_raw_buffer_store_b128(sent, rs, WS_Y + ((b * 128 + 64 + lane) * 128 + prod) * 16, 0, AUX_SC1);
    if (lane == 0) __hip_atomic_store(mlg + (b * NH + h) * NSPLIT + split, ~0ull, RLX_AGENT);
    if (lane < NSW) *(volatile LAS u64*)(L + L_ML + lane * 8) = ~0ull;
  }
  const int ngrp = xcd < 4 ? 4 : 3, gbase = xcd < 4 ? 4 * xcd : 16 + 3 * (xcd - 4);
  const bool has_ffn = (slot >> 3) < ngrp;
  const int chunk = (gbase + (slot >> 3)) * 8 + (slot & 7);
  const v4u x0 = ((const GAS v4u*)p.x)[lane], x1 = ((const GAS v4u*)(p.x + DM))[lane];
  const int cb = bid >> 7, cg = bid & 127;
  const v2u xres = *(const GAS v2u*)(p.x + cb * DM + 4 * cg);
  v4u wgt[8], wut[8], wdt[8];
  if (has_ffn) {
#pragma unroll
    for (int i = 0; i < 8; ++i) {
      wgt[i] = *(const GAS v4u*)(p.wg + (size_t)(8 * lane + i) * FF + 8 * chunk);
      wut[i] = *(const GAS v4u*)(p.wu + (size_t)(8 * lane + i) * FF + 8 * chunk);
    }
#pragma unroll
    for (int i = 0; i < 8; ++i) wdt[i] = ((const GAS v4u*)(p.wd + (size_t)(8 * chunk + i) * DM))[lane];
  }
  LDS_BARRIER();
  if (has_ffn) {
    unsigned xn[2][4];
    {
      float m0, d0, m1, d1;
      ln_stats(x0, m0, d0); ln_stats(x1, m1, d1);
#pragma unroll
      for (int j = 0; j < 4; ++j) {
        xn[0][j] = pack2(ln_apply(bf2f(x0[j] & 0xFFFFu), m0, d0), ln_apply(u2f(x0[j] & 0xFFFF0000u), m0, d0));
        xn[1][j] = pack2(ln_apply(bf2f(x1[j] & 0xFFFFu), m1, d1), ln_apply(u2f(x1[j] & 0xFFFF0000u), m1, d1));
      }
    }
    float v[32];
#pragma unroll
    for (int i = 0; i < 32; ++i) v[i] = 0.f;
#pragma unroll
    for (int a = 0; a < 4; ++a)
#pragma unroll
      for (int j = 0; j < 4; ++j) {
        const unsigned g0 = __builtin_amdgcn_perm(wgt[2 * a + 1][j], wgt[2 * a][j], 0x05040100u), g1 = __builtin_amdgcn_perm(wgt[2 * a + 1][j], wgt[2 * a][j], 0x07060302u);
        const unsigned u0 = __builtin_amdgcn_perm(wut[2 * a + 1][j], wut[2 * a][j], 0x05040100u), u1 = __builtin_amdgcn_perm(wut[2 * a + 1][j], wut[2 * a][j], 0x07060302u);
#pragma unroll
        for (int bb = 0; bb < 2; ++bb) {
          v[(bb * 8 + 2 * j) * 2 + 0] = dot2(g0, xn[bb][a], v[(bb * 8 + 2 * j) * 2 + 0]);
          v[(bb * 8 + 2 * j + 1) * 2 + 0] = dot2(g1, xn[bb][a], v[(bb * 8 + 2 * j + 1) * 2 + 0]);
          v[(bb * 8 + 2 * j) * 2 + 1] = dot2(u0, xn[bb][a], v[(bb * 8 + 2 * j) * 2 + 1]);
          v[(bb * 8 + 2 * j + 1) * 2 + 1] = dot2(u1, xn[bb][a], v[(bb * 8 + 2 * j + 1) * 2 + 1]);
        }
      }
#pragma unroll
    for (int i = 0; i < 16; ++i) v[i] = swap32_add(v[i], v[i + 16]);
#pragma unroll
    for (int i = 0; i < 8; ++i) v[i] = swap16_add(v[i], v[i + 8]);
#pragma unroll
    for (int i = 0; i < 4; ++i) { const float t1 = v[i] + dppf<ROR8>(v[i]), t2 = v[i + 4] + dppf<ROR8>(v[i + 4]); v[i] = (lane & 8) ? t2 : t1; }
#pragma unroll
    for (int i = 0; i < 2; ++i) { const float t1 = v[i] + dppf<HMIR>(v[i]), t2 = v[i + 2] + dppf<HMIR>(v[i + 2]); v[i] = (lane & 4) ? t2 : t1; }
#pragma unroll
    for (int i = 0; i < 2; ++i) { v[i] += dppf<XOR2>(v[i]); v[i] += dppf<XOR1>(v[i]); }
    const float hval = rbf(rbf(silu(rbf(v[0]))) * rbf(v[1]));
    const unsigned hb = f2bf(hval);
    const unsigned hpk = hb | (dppu<HMIR>(hb) << 16);
    float fo[2][8];
#pragma unroll
    for (int bb = 0; bb < 2; ++bb)
#pragma unroll
      for (int j = 0; j < 8; ++j) fo[bb][j] = 0.f;
#pragma unroll
    for (int a = 0; a < 4; ++a) {
      const unsigned h0 = __builtin_amdgcn_readlane(hpk, 8 * a), h1 = __builtin_amdgcn_readlane(hpk, 32 + 8 * a);
#pragma unroll
      for (int j = 0; j < 4; ++j) {
        const unsigned d0 = __builtin_amdgcn_perm(wdt[2 * a + 1][j], wdt[2 * a][j], 0x05040100u), d1 = __builtin_amdgcn_perm(wdt[2 * a + 1][j], wdt[2 * a][j], 0x07060302u);
        fo[0][2 * j] = dot2(d0, h0, fo[0][2 * j]); fo[0][2 * j + 1] = dot2(d1, h0, fo[0][2 * j + 1]);
        fo[1][2 * j] = dot2(d0, h1, fo[1][2 * j]); fo[1][2 * j + 1] = dot2(d1, h1, fo[1][2 * j + 1]);
      }
    }
#pragma unroll
    for (int bb = 0; bb < 2; ++bb)
#pragma unroll
      for (int hf = 0; hf < 2; ++hf) {
        const v4u o = {f2u(fo[bb][4 * hf]), f2u(fo[bb][4 * hf + 1]),
                       f2u(fo[bb][4 * hf + 2]), f2u(fo[bb][4 * hf + 3])};
        __builtin_amdgcn_raw_buffer_store_b128(o, rs, WS_F + ((bb * 128 + 2 * lane + hf) * NCHUNK + chunk) * 16, 0, AUX_SC1);
      }
  }
  asm volatile("s_waitcnt vmcnt(0)" ::: "memory");
  if (lane == 0) __hip_atomic_store(flags + bid, tag, RLX_AGENT);
  bool dead = false;
  for (unsigned spins = 0;; ++spins) {
    const v4u f0 = __builtin_amdgcn_raw_buffer_load_b128(rs, WS_FLAG + lane * 32, 0, AUX_SC1);
    const v4u f1 = __builtin_amdgcn_raw_buffer_load_b128(rs, WS_FLAG + lane * 32 + 16, 0, AUX_SC1);
    const unsigned tl = (unsigned)tag, th = (unsigned)(tag >> 32);
    const bool ok = f0[0] == tl && f0[1] == th && f0[2] == tl && f0[3] == th && f1[0] == tl && f1[1] == th && f1[2] == tl && f1[3] == th;
    if (__all(ok)) break;
    if (spins > SPIN_CAP) { dead = true; break; }
    __builtin_amdgcn_s_sleep(8);
  }
  __builtin_amdgcn_fence(__ATOMIC_ACQUIRE, "wavefront");
  if (bid == 0 && lane == 0) __hip_atomic_store(epoch, tag, RLX_AGENT);
  float g2[4];
  {
    f32x4 a = {0.f, 0.f, 0.f, 0.f};
#pragma unroll
    for (int i = 0; i < 4; ++i) {
      const int ch = lane + 64 * i;
      if (ch < NCHUNK) a += __builtin_bit_cast(f32x4, __builtin_amdgcn_raw_buffer_load_b128(rs, WS_F + ((cb * 128 + cg) * NCHUNK + ch) * 16, 0, AUX_SC1));
    }
#pragma unroll
    for (int j = 0; j < 4; ++j) g2[j] = rbf(silu(rbf(wave_sum(a[j]))));
  }
  {
    u64 mlw = ~0ull;
    for (unsigned spins = 0;; ++spins) {
      mlw = *(volatile LAS u64*)(L + L_ML + (lane & 7) * 8);
      if (__all(mlw != ~0ull)) break;
      if (spins > SPIN_CAP * 16) { dead = true; break; }
      __builtin_amdgcn_s_sleep(16);
    }
    const float mw = u2f((unsigned)mlw), lw = u2f((unsigned)(mlw >> 32));
    float mg = mw;
    mg = fmaxf(mg, dppf<XOR1>(mg)); mg = fmaxf(mg, dppf<XOR2>(mg)); mg = fmaxf(mg, dppf<HMIR>(mg));
    float lg = lw * __builtin_amdgcn_exp2f((mw - mg) * LOG2E);
    lg += dppf<XOR1>(lg); lg += dppf<XOR2>(lg); lg += dppf<HMIR>(lg);
    if (lane == 0) __hip_atomic_store(mlg + (b * NH + h) * NSPLIT + split, (u64)clean(mg) | ((u64)clean(lg) << 32), RLX_AGENT);
    u64 sib = ~0ull;
    for (unsigned spins = 0;; ++spins) {
      sib = __hip_atomic_load(mlg + (b * NH + h) * NSPLIT + (lane & 3), RLX_AGENT);
      if (__all((unsigned)sib != SENT && (unsigned)(sib >> 32) != SENT)) break;
      if (spins > SPIN_CAP) { dead = true; break; }
      __builtin_amdgcn_s_sleep(8);
    }
    const float ms = u2f((unsigned)sib), lsb = u2f((unsigned)(sib >> 32));
    float M = ms;
    M = fmaxf(M, dppf<XOR1>(M)); M = fmaxf(M, dppf<XOR2>(M));
    float Ls = lsb * __builtin_amdgcn_exp2f((ms - M) * LOG2E);
    Ls += dppf<XOR1>(Ls); Ls += dppf<XOR2>(Ls);
    if (lane < NSW) *(LAS float*)(L + L_SCALE + lane * 4) = __builtin_amdgcn_exp2f((mw - M) * LOG2E) / Ls;
  }
  LDS_BARRIER();
  LDS_BARRIER();
  {
    v4u y0, y1;
    for (unsigned spins = 0;; ++spins) {
      y0 = __builtin_amdgcn_raw_buffer_load_b128(rs, WS_Y + ((cb * 128 + cg) * 128 + lane) * 16, 0, AUX_SC1);
      y1 = __builtin_amdgcn_raw_buffer_load_b128(rs, WS_Y + ((cb * 128 + cg) * 128 + 64 + lane) * 16, 0, AUX_SC1);
      const bool ok = y0[0] != SENT && y0[1] != SENT && y0[2] != SENT && y0[3] != SENT && y1[0] != SENT && y1[1] != SENT && y1[2] != SENT && y1[3] != SENT;
      if (__all(ok)) break;
      if (spins > SPIN_CAP) { dead = true; break; }
      __builtin_amdgcn_s_sleep(1);
    }
    float o[4];
#pragma unroll
    for (int j = 0; j < 4; ++j) {
      const float s = rbf(wave_sum(u2f(y0[j]) + u2f(y1[j])));
      const float xr = (j & 1) ? u2f(xres[j >> 1] & 0xFFFF0000u) : bf2f(xres[j >> 1] & 0xFFFFu);
      o[j] = xr + rbf(s * g2[j]);
      if (dead) o[j] = __builtin_nanf("");
    }
    if (lane == 0) { const v2u ov = {pack2(o[0], o[1]), pack2(o[2], o[3])}; *(GAS v2u*)(p.out + cb * DM + 4 * cg) = ov; }
  }
}
}

extern "C" void kernel_launch(void* const* d_in, const int* in_sizes, int n_in, void* d_out, int out_size, void* d_ws,
                              size_t ws_size, hipStream_t stream) {
  Params p;
  p.x = (const unsigned short*)d_in[0];
  p.k = (const unsigned short*)d_in[1];
  p.v = (const unsigned short*)d_in[2];
  p.wq = (const unsigned short*)d_in[3];
  p.wo = (const unsigned short*)d_in[4];
  p.wg = (const unsigned short*)d_in[6];
  p.wu = (const unsigned short*)d_in[7];
  p.wd = (const unsigned short*)d_in[8];
  p.out = (unsigned short*)d_out;
  p.ws = (unsigned char*)d_ws;
  hipLaunchKernelGGL(fused_decode_kernel, dim3(NWG), dim3(NTHR), 0, stream, p);
}
```

```cpp
#include <hip/hip_runtime.h>
#include <hip/hip_bf16.h>
#include <stdint.h>
#include <type_traits>

#define GAS __attribute__((address_space(1)))
#define LAS __attribute__((address_space(3)))
typedef unsigned v4u __attribute__((ext_vector_type(4)));
typedef unsigned v2u __attribute__((ext_vector_type(2)));
typedef float f32x4 __attribute__((ext_vector_type(4)));
typedef float f32x2 __attribute__((ext_vector_type(2)));
typedef __bf16 bf16x2 __attribute__((ext_vector_type(2)));
typedef GAS unsigned long long gu64;
typedef unsigned long long u64;
typedef __amdgpu_buffer_rsrc_t rsrc_t;
#define RLX_AGENT __ATOMIC_RELAXED, __HIP_MEMORY_SCOPE_AGENT

namespace {
constexpr int NI = 3;
constexpr int NPRE = 16;
constexpr int DEPTH = 7;
constexpr int RN = 8;
constexpr unsigned PACE0 = 150, PACE = 1820;
constexpr unsigned T_FFN = 300;
constexpr unsigned T_WO = 1500;
constexpr unsigned T_FFN2 = 2000;
constexpr unsigned T_GATE = 3500;
constexpr int NB = 2, DM = 512, NH = 32, SK = 16384, DH = 128, FF = 1792;
constexpr int NWG = 256, NSW = 4, NTHR = 512;
constexpr int KEYS_WG = SK / 4, NT = KEYS_WG / 256;
constexpr int TSTEP = 256 * DH * 2;
constexpr int NFG = FF / 64, NFJ = NFG * 4;
constexpr unsigned SENT = 0xFFFFFFFFu;
constexpr int AUX_NT = 2, AUX_SC1 = 16, RSRC_W3 = 0x00020000;
constexpr unsigned CAP_SLOW = 1u << 13, CAP_FAST = 1u << 16, CAP_LDS = 1u << 18;
constexpr int WS_EPOCH = 0, WS_FLAG = 4096, WS_P1 = 8192;
constexpr int WS_QX = WS_P1 + NFJ * 1024;
constexpr int WS_F = WS_QX + NB * NH * 4 * 1024;
constexpr int WS_Y = WS_F + NB * 128 * NFG * 16;
constexpr int WS_END = WS_Y + NB * 128 * 128 * 16;
#define YOFF(b_, g_, p_) (WS_Y + (((b_) * 128 + (p_)) * 128 + (g_)) * 16)
constexpr int L_OP = 0;
constexpr int L_ML = 2048;
constexpr int L_H = L_ML + 128;
constexpr int L_QV = L_H + 256;
constexpr int L_SC = L_QV + 256;
constexpr int L_BYTES = L_SC + 4 * NT * 64 * 4;
constexpr float LOG2E = 1.4426950408889634f;
constexpr float QK_SCALE = 0.08838834764831845f;
static_assert(NI >= 1 && NI < NT, "prefetched tiles");

struct Params {
  const unsigned short* x;
  const unsigned short* k;
  const unsigned short* v;
  const unsigned short* wq;
  const unsigned short* wo;
  const unsigned short* wg;
  const unsigned short* wu;
  const unsigned short* wd;
  unsigned short* out;
  unsigned char* ws;
};

__device__ __forceinline__ float u2f(unsigned u) { return __uint_as_float(u); }
__device__ __forceinline__ unsigned f2u(float f) { return __float_as_uint(f); }
__device__ __forceinline__ float dot2(unsigned a, unsigned b, float c) {
  return __builtin_amdgcn_fdot2_f32_bf16(__builtin_bit_cast(bf16x2, a), __builtin_bit_cast(bf16x2, b), c, false);
}
template <int CTRL> __device__ __forceinline__ float dppf(float x) {
  return __builtin_bit_cast(float, __builtin_amdgcn_mov_dpp(__builtin_bit_cast(int, x), CTRL, 0xf, 0xf, true));
}
template <int CTRL> __device__ __forceinline__ unsigned dppu(unsigned x) {
  return (unsigned)__builtin_amdgcn_mov_dpp((int)x, CTRL, 0xf, 0xf, true);
}
constexpr int XOR1 = 0xB1, XOR2 = 0x4E, HMIR = 0x141, MIR = 0x140, ROR8 = 0x128, BCAST = 0x150;
__device__ __forceinline__ float bf2f(unsigned bits16) { return u2f(bits16 << 16); }
__device__ __forceinline__ unsigned f2bf(float f) { return (unsigned)__builtin_bit_cast(unsigned short, (__bf16)f); }
__device__ __forceinline__ float rbf(float f) { return bf2f(f2bf(f)); }
__device__ __forceinline__ unsigned pack2(float lo, float hi) {
  bf16x2 r = {(__bf16)lo, (__bf16)hi};
  return __builtin_bit_cast(unsigned, r);
}
__device__ __forceinline__ float swap32_add(float a, float b) {
  auto s = __builtin_amdgcn_permlane32_swap(f2u(a), f2u(b), false, false);
  return u2f(s[0]) + u2f(s[1]);
}
__device__ __forceinline__ float swap16_add(float a, float b) {
  auto s = __builtin_amdgcn_permlane16_swap(f2u(a), f2u(b), false, false);
  return u2f(s[0]) + u2f(s[1]);
}
__device__ __forceinline__ float swap32_max(float a) {
  auto s = __builtin_amdgcn_permlane32_swap(f2u(a), f2u(a), false, false);
  return fmaxf(u2f(s[0]), u2f(s[1]));
}
__device__ __forceinline__ float swap16_max(float a) {
  auto s = __builtin_amdgcn_permlane16_swap(f2u(a), f2u(a), false, false);
  return fmaxf(u2f(s[0]), u2f(s[1]));
}
__device__ __forceinline__ float wave_sum(float x) {
  x += dppf<XOR1>(x); x += dppf<XOR2>(x); x += dppf<HMIR>(x); x += dppf<MIR>(x);
  x = swap16_add(x, x); return swap32_add(x, x);
}
__device__ __forceinline__ float wave_max(float x) {
  x = fmaxf(x, dppf<XOR1>(x)); x = fmaxf(x, dppf<XOR2>(x)); x = fmaxf(x, dppf<HMIR>(x)); x = fmaxf(x, dppf<MIR>(x));
  x = swap16_max(x); return swap32_max(x);
}
__device__ __forceinline__ unsigned rnd24(float v) {
  const unsigned u = f2u(v);
  return (v != v) ? 0x7FC00000u : ((u + 0x80u) & 0xFFFFFF00u);
}
__device__ __forceinline__ float silu(float g) { return g / (1.0f + __expf(-g)); }
__device__ __forceinline__ unsigned clean(float v) {
  const unsigned u = f2u(v);
  return u == SENT ? 0x7FC00000u : u;
}
__device__ __forceinline__ rsrc_t mk_rsrc(const void* base, unsigned bytes) { return __builtin_amdgcn_make_buffer_rsrc((void*)base, 0, bytes, RSRC_W3); }
__device__ __forceinline__ v4u ld_sc1(rsrc_t rs, int off) { return __builtin_amdgcn_raw_buffer_load_b128(rs, off, 0, AUX_SC1); }
__device__ __forceinline__ void st_sc1(v4u v, rsrc_t rs, int off) { __builtin_amdgcn_raw_buffer_store_b128(v, rs, off, 0, AUX_SC1); }
__device__ __forceinline__ bool has_sent(v4u v) { return v[0] == SENT || v[1] == SENT || v[2] == SENT || v[3] == SENT; }
__device__ __forceinline__ void wait_until(u64 t0, unsigned ticks) {
  for (unsigned i = 0; i < CAP_SLOW && __builtin_amdgcn_s_memrealtime() - t0 < ticks; ++i) __builtin_amdgcn_s_sleep(16);
}
__device__ __forceinline__ int fresh_lane() {
  int l;
  asm volatile("v_mbcnt_lo_u32_b32 %0, -1, 0\n\tv_mbcnt_hi_u32_b32 %0, -1, %0" : "=v"(l));
  return l;
}
#define SB() __builtin_amdgcn_sched_barrier(0)
#define PIN8(a) asm volatile("" : "+v"(a[0]), "+v"(a[1]), "+v"(a[2]), "+v"(a[3]), "+v"(a[4]), "+v"(a[5]), "+v"(a[6]), "+v"(a[7]))
#define LDS_BARRIER() do { asm volatile("s_waitcnt lgkmcnt(0)" ::: "memory"); __builtin_amdgcn_s_barrier(); asm volatile("" ::: "memory"); } while (0)
#define PACE_WAIT(u_) do { const u64 due_ = ts + PACE0 + (((u64)(u_) * PACE) >> 4); while (__builtin_amdgcn_s_memrealtime() < due_) __builtin_amdgcn_s_sleep(1); } while (0)
#define ORDER_BEHIND_POLL() __builtin_amdgcn_fence(__ATOMIC_ACQUIRE, "wavefront")

__device__ __forceinline__ void ln_stats(v4u xv, float& mean_bf, float& den_bf) {
  float e[8];
#pragma unroll
  for (int j = 0; j < 4; ++j) { e[2 * j] = bf2f(xv[j] & 0xFFFFu); e[2 * j + 1] = u2f(xv[j] & 0xFFFF0000u); }
  float s = 0.f;
#pragma unroll
  for (int j = 0; j < 8; ++j) s += e[j];
  const float mean = wave_sum(s) * (1.0f / DM);
  float q = 0.f;
#pragma unroll
  for (int j = 0; j < 8; ++j) { const float d = e[j] - mean; q = fmaf(d, d, q); }
  const float var = wave_sum(q) * (1.0f / DM);
  mean_bf = rbf(mean);
  den_bf = rbf(sqrtf(rbf(rbf(var) + 1e-5f)));
}
__device__ __forceinline__ float ln_apply(float x, float mean_bf, float den_bf) { return rbf(rbf(x - mean_bf) / den_bf); }

__device__ __forceinline__ f32x2 rows_reduce8(const float (&a)[8]) {
  float t[4];
#pragma unroll
  for (int j = 0; j < 4; ++j) t[j] = swap32_add(a[j], a[j + 4]);
  f32x2 r;
  r[0] = swap16_add(t[0], t[2]);
  r[1] = swap16_add(t[1], t[3]);
  return r;
}

template <int I> __device__ __forceinline__ void pv_step(const v4u& cur, unsigned plo, unsigned phi, float (&acc)[8]) {
  const unsigned bl = dppu<BCAST + I>(plo), bh = dppu<BCAST + I>(phi);
#pragma unroll
  for (int j = 0; j < 4; ++j) {
    acc[2 * j] = dot2(cur[j], bl, acc[2 * j]);
    acc[2 * j + 1] = dot2(cur[j], bh, acc[2 * j + 1]);
  }
}
template <int I, int N> struct Unroll {
  template <class Fn> static __device__ __forceinline__ void run(Fn&& f) {
    f(std::integral_constant<int, I>{});
    Unroll<I + 1, N>::run(f);
  }
};
template <int N> struct Unroll<N, N> {
  template <class Fn> static __device__ __forceinline__ void run(Fn&&) {}
};

__global__ void __launch_bounds__(NTHR, 2) fused_decode_kernel(Params p) {
  __shared__ __attribute__((aligned(16))) unsigned char smem[L_BYTES];
  LAS unsigned char* L = (LAS unsigned char*)smem;
  const int tid = threadIdx.x, lane = tid & 63, w = __builtin_amdgcn_readfirstlane(tid >> 6);
  const int bid = blockIdx.x, xl = bid & 7, slot = bid >> 3;
  const int h = (slot >> 3) * 8 + xl, b = (slot >> 2) & 1, mem = slot & 3, grp = b * NH + h;
  const int prod = h * 4 + mem;
  const rsrc_t rs = mk_rsrc(p.ws, WS_END);
  const int r = lane >> 4, c = lane & 15;

  if (w < NSW) {
    const u64 ts = __builtin_amdgcn_s_memrealtime();
    const size_t kvbase = ((size_t)grp * SK + (size_t)mem * KEYS_WG) * DH * 2;
    const unsigned char* kb = (const unsigned char*)p.k + kvbase;
    const unsigned char* vb = (const unsigned char*)p.v + kvbase;
    const unsigned voff = 16 * lane + 1024 * w;
    auto ldp = [&](rsrc_t rr, int i) -> v4u { return __builtin_amdgcn_raw_buffer_load_b128(rr, voff, i * 4096, AUX_NT); };
    v4u ini[16 * NI];
    {
      const rsrc_t rk0 = mk_rsrc(kb, NI * TSTEP);
#pragma unroll
      for (int i = 0; i < NPRE; ++i) ini[i] = __builtin_amdgcn_raw_buffer_load_b128(rk0, voff, (i >> 4) * TSTEP + (i & 15) * 4096, AUX_NT);
    }
    SB();
    __builtin_amdgcn_s_barrier();
    SB();
#pragma unroll
    for (int t = 0; t < NI; ++t) {
      const rsrc_t rk = mk_rsrc(kb + (size_t)t * TSTEP, TSTEP);
#pragma unroll
      for (int i = 0; i < 16; ++i) if (16 * t + i >= NPRE) ini[16 * t + i] = ldp(rk, i);
    }
    SB();
    LDS_BARRIER();
    unsigned qpk[4];
    { const v4u qv = *(LAS v4u*)(L + L_QV + c * 16); qpk[0] = qv[0]; qpk[1] = qv[1]; qpk[2] = qv[2]; qpk[3] = qv[3]; }
    LAS unsigned* scl = (LAS unsigned*)(L + L_SC) + w * (NT * 64) + lane;
    float mrun = -INFINITY;
    v4u ring[RN];
#define QK4(d_, v_) float d_ = dot2((v_)[0], qpk[0], 0.f); d_ = dot2((v_)[1], qpk[1], d_); d_ = dot2((v_)[2], qpk[2], d_); d_ = dot2((v_)[3], qpk[3], d_); asm volatile("" : "+v"(d_))
#define TR_STEP(HALF, CTRL, BIT) _Pragma("unroll") for (int hh = 0; hh < (HALF); ++hh) { \
          const float lo_ = x[hh], hi_ = x[hh + (HALF)]; const float t1_ = lo_ + dppf<CTRL>(lo_), t2_ = hi_ + dppf<CTRL>(hi_); \
          x[hh] = (c & (BIT)) ? t2_ : t1_; }
#define SCORE(t_) do { TR_STEP(8, ROR8, 8) TR_STEP(4, HMIR, 4) TR_STEP(2, XOR2, 2) TR_STEP(1, XOR1, 1) \
          const float s_ = rbf(rbf(x[0]) * QK_SCALE); \
          mrun = fmaxf(mrun, s_); scl[(t_) * 64] = f2u(s_); } while (0)
    {
      const rsrc_t rn = mk_rsrc(kb + (size_t)NI * TSTEP, TSTEP);
#pragma unroll
      for (int t = 0; t < NI; ++t) {
        float x[16];
#pragma unroll
        for (int i = 0; i < 16; ++i) { QK4(d, ini[16 * t + i]); x[i] = d; SB(); if (t == 0 && i < DEPTH) ring[i] = ldp(rn, i); SB(); }
        SCORE(t);
      }
    }
#pragma unroll 1
    for (int t = NI; t < NT; ++t) {
      const rsrc_t rc = mk_rsrc(kb + (size_t)t * TSTEP, TSTEP);
      const rsrc_t rn = mk_rsrc(t + 1 < NT ? kb + (size_t)(t + 1) * TSTEP : vb, TSTEP);
      float x[16];
#pragma unroll
      for (int i = 0; i < 16; ++i) {
        if (i == 0 || i == 8) PACE_WAIT(2 * t + (i >> 3));
        QK4(d, ring[i & (RN - 1)]);
        x[i] = d;
        SB();
        ring[(i + DEPTH) & (RN - 1)] = i + DEPTH < 16 ? ldp(rc, i + DEPTH) : ldp(rn, i + DEPTH - 16);
        SB();
      }
      SCORE(t);
    }
#undef SCORE
#undef TR_STEP
#undef QK4
    const float mw = wave_max(mrun);
    float lsum = 0.f;
    {
      float sv[NT];
#pragma unroll
      for (int t = 0; t < NT; ++t) sv[t] = u2f(scl[t * 64]);
#pragma unroll
      for (int t = 0; t < NT; ++t) {
        const float e = __builtin_amdgcn_exp2f((sv[t] - mw) * LOG2E);
        lsum += e;
        scl[t * 64] = f2bf(e);
      }
      const float lw = wave_sum(lsum);
      if (fresh_lane() == 0) { const f32x2 ml = {mw, lw}; *(volatile LAS f32x2*)(L + L_ML + w * 8) = ml; }
    }
    float acc[8];
#pragma unroll
    for (int j = 0; j < 8; ++j) acc[j] = 0.f;
#pragma unroll 1
    for (int t = 0; t < NT - 1; ++t) {
      const rsrc_t rc = mk_rsrc(vb + (size_t)t * TSTEP, TSTEP), rn = mk_rsrc(vb + (size_t)(t + 1) * TSTEP, TSTEP);
      const unsigned plo = scl[t * 64], phi = plo << 16;
      Unroll<0, 16>::run([&](auto I) {
        constexpr int i = I.value;
        if (i == 0 || i == 8) PACE_WAIT(2 * (NT + t) + (i >> 3));
        pv_step<i>(ring[i & (RN - 1)], plo, phi, acc);
        PIN8(acc);
        SB();
        ring[(i + DEPTH) & (RN - 1)] = i + DEPTH < 16 ? ldp(rc, i + DEPTH) : ldp(rn, i + DEPTH - 16);
        SB();
      });
    }
    {
      const rsrc_t rc = mk_rsrc(vb + (size_t)(NT - 1) * TSTEP, TSTEP);
      const unsigned plo = scl[(NT - 1) * 64], phi = plo << 16;
      Unroll<0, 16>::run([&](auto I) {
        constexpr int i = I.value;
        pv_step<i>(ring[i & (RN - 1)], plo, phi, acc);
        PIN8(acc);
        SB();
        if (i + DEPTH < 16) ring[(i + DEPTH) & (RN - 1)] = ldp(rc, i + DEPTH);
        SB();
      });
    }
    {
      const int ln = fresh_lane();
      const f32x2 op = rows_reduce8(acc);
      *(LAS f32x2*)(L + L_OP + (w * 128 + 8 * (ln & 15) + 2 * (ln >> 4)) * 4) = op;
    }
    LDS_BARRIER();
    return;
  }

  __builtin_amdgcn_s_setprio(3);
  const u64 t0 = __builtin_amdgcn_s_memrealtime();
  const int hw = w - NSW;
  bool dead = false;
  u64 tag = 0;
  gu64* const epoch = (gu64*)(p.ws + WS_EPOCH);
  gu64* const flags = (gu64*)(p.ws + WS_FLAG);
  if (hw == 0) {
    const rsrc_t rx = mk_rsrc(p.x + b * DM, DM * 2);
    const v2u ep = __builtin_amdgcn_raw_buffer_load_b64(rs, WS_EPOCH, 0, AUX_SC1);
    const v4u xv = __builtin_amdgcn_raw_buffer_load_b128(rx, lane * 16, 0, 0);
    const unsigned xp = __builtin_amdgcn_raw_buffer_load_b32(rx, (64 * mem + lane) * 4, 0, 0);
    v4u wt[32];
    {
      const rsrc_t rq = mk_rsrc(p.wq + (size_t)(128 * mem) * (NH * DH) + h * DH, 128 * NH * DH * 2);
      const unsigned vo = (32 * r) * (NH * DH * 2) + 16 * c;
#pragma unroll
      for (int i = 0; i < 32; ++i) wt[i] = __builtin_amdgcn_raw_buffer_load_b128(rq, vo, i * (NH * DH * 2), AUX_NT);
    }
    SB();
    __builtin_amdgcn_s_barrier();
    SB();
    tag = (((u64)ep[1] << 32) | ep[0]) + 1ull;
    float mean_bf, den_bf;
    ln_stats(xv, mean_bf, den_bf);
    const unsigned xn = pack2(ln_apply(bf2f(xp & 0xFFFFu), mean_bf, den_bf), ln_apply(u2f(xp & 0xFFFF0000u), mean_bf, den_bf));
    float acc[8];
#pragma unroll
    for (int j = 0; j < 8; ++j) acc[j] = 0.f;
    Unroll<0, 16>::run([&](auto A) {
      constexpr int a = A.value;
      const unsigned xa = dppu<BCAST + a>(xn);
#pragma unroll
      for (int j = 0; j < 4; ++j) {
        const unsigned e0 = wt[2 * a][j], e1 = wt[2 * a + 1][j];
        acc[2 * j] = dot2(__builtin_amdgcn_perm(e1, e0, 0x05040100u), xa, acc[2 * j]);
        acc[2 * j + 1] = dot2(__builtin_amdgcn_perm(e1, e0, 0x07060302u), xa, acc[2 * j + 1]);
      }
    });
    const f32x2 qp = rows_reduce8(acc);
    const unsigned tl = (unsigned)tag, th = (unsigned)(tag >> 32) & 0xFFu;
    st_sc1(v4u{tl, rnd24(qp[0]) | th, tl, rnd24(qp[1]) | th}, rs, WS_QX + (grp * 4 + mem) * 1024 + (4 * c + r) * 16);
    {
      v4u g[4];
      for (unsigned spins = 0;; ++spins) {
        bool ok = true;
#pragma unroll
        for (int m = 0; m < 4; ++m) {
          g[m] = ld_sc1(rs, WS_QX + (grp * 4 + m) * 1024 + lane * 16);
          ok = ok && g[m][0] == tl && (g[m][1] & 0xFFu) == th && g[m][2] == tl && (g[m][3] & 0xFFu) == th;
        }
        if (__all(ok)) break;
        if (spins > CAP_FAST) { dead = true; break; }
        __builtin_amdgcn_s_sleep(1);
      }
      const float q0 = (u2f(g[0][1] & 0xFFFFFF00u) + u2f(g[1][1] & 0xFFFFFF00u)) + (u2f(g[2][1] & 0xFFFFFF00u) + u2f(g[3][1] & 0xFFFFFF00u));
      const float q1 = (u2f(g[0][3] & 0xFFFFFF00u) + u2f(g[1][3] & 0xFFFFFF00u)) + (u2f(g[2][3] & 0xFFFFFF00u) + u2f(g[3][3] & 0xFFFFFF00u));
      *(LAS unsigned*)(L + L_QV + lane * 4) = pack2(q0, q1);
    }
  } else {
    if (hw == 1 && lane < NSW) *(volatile LAS u64*)(L + L_ML + lane * 8) = ~0ull;
    __builtin_amdgcn_s_barrier();
  }
  LDS_BARRIER();
  __builtin_amdgcn_s_setprio(0);
  const int cb = xl >> 2, cg = ((xl & 3) * 2 + (slot >> 4)) * 16 + (slot & 15);
  const int job = slot * 4 + (xl >> 1), fg = job >> 2, kq = job & 3;
  const bool has_ffn = (xl & 1) == 0 && job < NFJ;
#define JOB_OWNER(j_) (((j_) >> 2) * 8 + ((j_) & 3) * 2)
  if (hw == 0) {
    wait_until(t0, T_FFN);
    const int rr = lane >> 3, cc = lane & 7;
    {
      const v4u sent = {SENT, SENT, SENT, SENT};
      st_sc1(sent, rs, YOFF(b, lane, prod));
      st_sc1(sent, rs, YOFF(b, 64 + lane, prod));
      if (has_ffn) st_sc1(sent, rs, WS_F + ((fg * 8 + 2 * kq) * 32 + lane) * 16);
    }
    if (has_ffn) {
      const v4u x0 = ((const GAS v4u*)p.x)[lane], x1 = ((const GAS v4u*)(p.x + DM))[lane];
      float m0, d0, m1, d1;
      ln_stats(x0, m0, d0); ln_stats(x1, m1, d1);
      const rsrc_t rg = mk_rsrc(p.wg, DM * FF * 2), ru = mk_rsrc(p.wu, DM * FF * 2);
      float v[32];
#pragma unroll
      for (int i = 0; i < 32; ++i) v[i] = 0.f;
#pragma unroll 1
      for (int hf = 0; hf < 2; ++hf) {
        const int kb8 = 2 * kq + hf;
        const v4u s0 = ((const GAS v4u*)p.x)[8 * kb8 + rr], s1 = ((const GAS v4u*)(p.x + DM))[8 * kb8 + rr];
        v4u wgt[8], wut[8];
        const unsigned vo = (64 * kb8 + 8 * rr) * (FF * 2) + (64 * fg + 8 * cc) * 2;
#pragma unroll
        for (int i = 0; i < 8; ++i) {
          wgt[i] = __builtin_amdgcn_raw_buffer_load_b128(rg, vo, i * (FF * 2), AUX_NT);
          wut[i] = __builtin_amdgcn_raw_buffer_load_b128(ru, vo, i * (FF * 2), AUX_NT);
        }
        unsigned xn[2][4];
#pragma unroll
        for (int j = 0; j < 4; ++j) {
          xn[0][j] = pack2(ln_apply(bf2f(s0[j] & 0xFFFFu), m0, d0), ln_apply(u2f(s0[j] & 0xFFFF0000u), m0, d0));
          xn[1][j] = pack2(ln_apply(bf2f(s1[j] & 0xFFFFu), m1, d1), ln_apply(u2f(s1[j] & 0xFFFF0000u), m1, d1));
        }
#pragma unroll
        for (int a = 0; a < 4; ++a)
#pragma unroll
          for (int j = 0; j < 4; ++j) {
            const unsigned g0 = __builtin_amdgcn_perm(wgt[2 * a + 1][j], wgt[2 * a][j], 0x05040100u), g1 = __builtin_amdgcn_perm(wgt[2 * a + 1][j], wgt[2 * a][j], 0x07060302u);
            const unsigned u0 = __builtin_amdgcn_perm(wut[2 * a + 1][j], wut[2 * a][j], 0x05040100u), u1 = __builtin_amdgcn_perm(wut[2 * a + 1][j], wut[2 * a][j], 0x07060302u);
#pragma unroll
            for (int bb = 0; bb < 2; ++bb) {
              v[(bb * 8 + 2 * j) * 2 + 0] = dot2(g0, xn[bb][a], v[(bb * 8 + 2 * j) * 2 + 0]);
              v[(bb * 8 + 2 * j + 1) * 2 + 0] = dot2(g1, xn[bb][a], v[(bb * 8 + 2 * j + 1) * 2 + 0]);
              v[(bb * 8 + 2 * j) * 2 + 1] = dot2(u0, xn[bb][a], v[(bb * 8 + 2 * j) * 2 + 1]);
              v[(bb * 8 + 2 * j + 1) * 2 + 1] = dot2(u1, xn[bb][a], v[(bb * 8 + 2 * j + 1) * 2 + 1]);
            }
          }
      }
#pragma unroll
      for (int i = 0; i < 16; ++i) v[i] = swap32_add(v[i], v[i + 16]);
#pragma unroll
      for (int i = 0; i < 8; ++i) v[i] = swap16_add(v[i], v[i + 8]);
#pragma unroll
      for (int i = 0; i < 4; ++i) { const float t1 = v[i] + dppf<ROR8>(v[i]), t2 = v[i + 4] + dppf<ROR8>(v[i + 4]); v[i] = (lane & 8) ? t2 : t1; }
      st_sc1(v4u{f2u(v[0]), f2u(v[1]), f2u(v[2]), f2u(v[3])}, rs, WS_P1 + job * 1024 + lane * 16);
    }
    asm volatile("s_waitcnt vmcnt(0)" ::: "memory");
    if (lane == 0) __hip_atomic_store(flags + bid, tag, RLX_AGENT);
  }
  wait_until(t0, T_WO);
  v4u wo[32];
  {
    const rsrc_t ro = mk_rsrc(p.wo + (size_t)(h * DH) * DM + 128 * hw, DH * DM * 2);
    const unsigned vo = (32 * r) * (DM * 2) + 16 * c;
#pragma unroll
    for (int i = 0; i < 32; ++i) wo[i] = __builtin_amdgcn_raw_buffer_load_b128(ro, vo, i * (DM * 2), AUX_NT);
#pragma unroll
    for (int a = 0; a < 16; ++a)
#pragma unroll
      for (int j = 0; j < 4; ++j) {
        const unsigned e0 = wo[2 * a][j], e1 = wo[2 * a + 1][j];
        wo[2 * a][j] = __builtin_amdgcn_perm(e1, e0, 0x05040100u);
        wo[2 * a + 1][j] = __builtin_amdgcn_perm(e1, e0, 0x07060302u);
      }
#pragma unroll
    for (int i = 0; i < 32; ++i) asm volatile("" : "+v"(wo[i]));
  }
  float g2[4] = {0.f, 0.f, 0.f, 0.f};
  v2u xres = {0u, 0u};
  if (hw == 0) {
    const int rr = lane >> 3, cc = lane & 7;
    xres = *(const GAS v2u*)(p.x + cb * DM + 4 * cg);
    wait_until(t0, T_FFN2);
    if (has_ffn) {
      for (unsigned spins = 0;; ++spins) {
        const u64 f = __hip_atomic_load(flags + JOB_OWNER(fg * 4 + (lane & 3)), RLX_AGENT);
        if (__all(f == tag)) break;
        if (spins > CAP_SLOW) { dead = true; break; }
        __builtin_amdgcn_s_sleep(64);
      }
      ORDER_BEHIND_POLL();
      f32x4 a = {0.f, 0.f, 0.f, 0.f};
#pragma unroll
      for (int i = 0; i < 4; ++i) a += __builtin_bit_cast(f32x4, ld_sc1(rs, WS_P1 + (fg * 4 + i) * 1024 + lane * 16));
      const float h0 = rbf(rbf(silu(rbf(a[0]))) * rbf(a[1])), h1 = rbf(rbf(silu(rbf(a[2]))) * rbf(a[3]));
      *(LAS unsigned*)(L + L_H + ((lane >> 5) * 32 + 4 * cc + 2 * ((lane >> 4) & 1) + ((lane >> 3) & 1)) * 4) = pack2(h0, h1);
      asm volatile("s_waitcnt lgkmcnt(0)" ::: "memory");
      const v4u hp0 = *(LAS v4u*)(L + L_H + (4 * rr) * 4), hp1 = *(LAS v4u*)(L + L_H + (32 + 4 * rr) * 4);
#pragma unroll 1
      for (int hf = 0; hf < 2; ++hf) {
        const int kb8 = 2 * kq + hf;
        v4u wdt[8];
        {
          const rsrc_t rd = mk_rsrc(p.wd, FF * DM * 2);
          const unsigned vd = (64 * fg + 8 * rr) * (DM * 2) + (64 * kb8 + 8 * cc) * 2;
#pragma unroll
          for (int i = 0; i < 8; ++i) wdt[i] = __builtin_amdgcn_raw_buffer_load_b128(rd, vd, i * (DM * 2), AUX_NT);
        }
        float fo[16];
#pragma unroll
        for (int j = 0; j < 16; ++j) fo[j] = 0.f;
#pragma unroll
        for (int a2 = 0; a2 < 4; ++a2)
#pragma unroll
          for (int j = 0; j < 4; ++j) {
            const unsigned d0 = __builtin_amdgcn_perm(wdt[2 * a2 + 1][j], wdt[2 * a2][j], 0x05040100u), d1 = __builtin_amdgcn_perm(wdt[2 * a2 + 1][j], wdt[2 * a2][j], 0x07060302u);
            fo[2 * j] = dot2(d0, hp0[a2], fo[2 * j]); fo[2 * j + 1] = dot2(d1, hp0[a2], fo[2 * j + 1]);
            fo[8 + 2 * j] = dot2(d0, hp1[a2], fo[8 + 2 * j]); fo[8 + 2 * j + 1] = dot2(d1, hp1[a2], fo[8 + 2 * j + 1]);
          }
#pragma unroll
        for (int i = 0; i < 8; ++i) fo[i] = swap32_add(fo[i], fo[i + 8]);
#pragma unroll
        for (int i = 0; i < 4; ++i) fo[i] = swap16_add(fo[i], fo[i + 4]);
        v4u o;
#pragma unroll
        for (int i = 0; i < 4; ++i) o[i] = clean(fo[i] + dppf<ROR8>(fo[i]));
        if ((lane & 8) == 0) st_sc1(o, rs, WS_F + ((fg * 8 + kb8) * 32 + (lane >> 5) * 16 + 2 * cc + ((lane >> 4) & 1)) * 16);
      }
    }
    wait_until(t0, T_GATE);
    for (unsigned spins = 0;; ++spins) {
      const v4u f0 = ld_sc1(rs, WS_FLAG + lane * 32), f1 = ld_sc1(rs, WS_FLAG + lane * 32 + 16);
      const unsigned tl = (unsigned)tag, th = (unsigned)(tag >> 32);
      const bool ok = f0[0] == tl && f0[1] == th && f0[2] == tl && f0[3] == th && f1[0] == tl && f1[1] == th && f1[2] == tl && f1[3] == th;
      if (__all(ok)) break;
      if (spins > CAP_SLOW) { dead = true; break; }
      __builtin_amdgcn_s_sleep(64);
    }
    ORDER_BEHIND_POLL();
    if (bid == 0 && lane == 0) __hip_atomic_store(epoch, tag, RLX_AGENT);
    {
      v4u fv;
      for (unsigned spins = 0;; ++spins) {
        fv = ld_sc1(rs, WS_F + (((lane < NFG ? lane : 0) * 8 + (cg >> 4)) * 32 + cb * 16 + (cg & 15)) * 16);
        if (__all(!has_sent(fv))) break;
        if (spins > CAP_SLOW) { dead = true; break; }
        __builtin_amdgcn_s_sleep(64);
      }
#pragma unroll
      for (int j = 0; j < 4; ++j) g2[j] = rbf(silu(rbf(wave_sum(lane < NFG ? u2f(fv[j]) : 0.f))));
    }
  }
  float sw0, sw1, sw2, sw3, lam;
  {
    f32x4 ma, mb;
    for (unsigned spins = 0;; ++spins) {
      ma = *(volatile LAS f32x4*)(L + L_ML); mb = *(volatile LAS f32x4*)(L + L_ML + 16);
      if ((f2u(ma[1]) != SENT && f2u(ma[3]) != SENT && f2u(mb[1]) != SENT && f2u(mb[3]) != SENT) || spins > CAP_LDS) break;
      __builtin_amdgcn_s_sleep(32);
    }
    const float mg = fmaxf(fmaxf(ma[0], ma[2]), fmaxf(mb[0], mb[2]));
    const float e0 = __builtin_amdgcn_exp2f((ma[0] - mg) * LOG2E), e1 = __builtin_amdgcn_exp2f((ma[2] - mg) * LOG2E);
    const float e2 = __builtin_amdgcn_exp2f((mb[0] - mg) * LOG2E), e3 = __builtin_amdgcn_exp2f((mb[2] - mg) * LOG2E);
    const float lg = fmaf(ma[1], e0, fmaf(ma[3], e1, fmaf(mb[1], e2, mb[3] * e3)));
    const float il = 1.0f / lg;
    sw0 = e0 * il; sw1 = e1 * il; sw2 = e2 * il; sw3 = e3 * il;
    lam = fmaf(mg, LOG2E, __builtin_amdgcn_logf(lg));
    asm volatile("" : "+v"(sw0), "+v"(sw1), "+v"(sw2), "+v"(sw3), "+v"(lam));
  }
  __builtin_amdgcn_s_setprio(3);
  LDS_BARRIER();
  {
    const int ln = fresh_lane();
    const f32x2 p0 = *(LAS f32x2*)(L + L_OP + (0 * 128 + 2 * ln) * 4), p1 = *(LAS f32x2*)(L + L_OP + (1 * 128 + 2 * ln) * 4);
    const f32x2 p2 = *(LAS f32x2*)(L + L_OP + (2 * 128 + 2 * ln) * 4), p3 = *(LAS f32x2*)(L + L_OP + (3 * 128 + 2 * ln) * 4);
    const float o0 = fmaf(p0[0], sw0, fmaf(p1[0], sw1, fmaf(p2[0], sw2, p3[0] * sw3))), o1 = fmaf(p0[1], sw0, fmaf(p1[1], sw1, fmaf(p2[1], sw2, p3[1] * sw3)));
    const unsigned opk = pack2(o0, o1);
    float y[8];
#pragma unroll
    for (int j = 0; j < 8; ++j) y[j] = 0.f;
    Unroll<0, 16>::run([&](auto A) {
      const unsigned oa = dppu<BCAST + A.value>(opk);
#pragma unroll
      for (int j = 0; j < 4; ++j) {
        y[2 * j] = dot2(wo[2 * A.value][j], oa, y[2 * j]);
        y[2 * j + 1] = dot2(wo[2 * A.value + 1][j], oa, y[2 * j + 1]);
      }
    });
    unsigned yq[4];
#pragma unroll
    for (int j = 0; j < 4; ++j) { float t_ = swap32_add(y[j], y[j + 4]); t_ = swap16_add(t_, t_); yq[j] = rnd24(t_); }
    const v4u o = {yq[0] | (yq[3] >> 24), yq[1] | ((yq[3] >> 16) & 0xFFu), yq[2] | ((yq[3] >> 8) & 0xFFu), clean(lam)};
    if ((ln & 16) == 0) st_sc1(o, rs, YOFF(b, 32 * hw + 2 * (ln & 15) + (ln >> 5), prod));
  }
  if (hw != 0) return;
  {
    const int ln = fresh_lane();
    const int jo = 2 * ((ln >> 4) & 1) + (ln >> 5);
    const float g2l = jo == 0 ? g2[0] : jo == 1 ? g2[1] : jo == 2 ? g2[2] : g2[3];
    const float xl_ = (jo & 1) ? u2f(xres[jo >> 1] & 0xFFFF0000u) : bf2f(xres[jo >> 1] & 0xFFFFu);
    const int yoff0 = YOFF(cb, cg, 2 * ln), yoff1 = YOFF(cb, cg, 2 * ln + 1);
    v4u y0 = {SENT, SENT, SENT, SENT}, y1 = {SENT, SENT, SENT, SENT};
    for (unsigned spins = 0;; ++spins) {
      const bool miss0 = has_sent(y0), miss1 = has_sent(y1);
      if (!__any(miss0 || miss1)) break;
      if (spins > CAP_FAST) { dead = true; break; }
      if (spins) __builtin_amdgcn_s_sleep(2);
      if (miss0) y0 = ld_sc1(rs, yoff0);
      if (miss1) y1 = ld_sc1(rs, yoff1);
    }
    const float m0 = u2f(y0[3]), m1 = u2f(y1[3]);
    float M = fmaxf(m0, m1);
    M = fmaxf(M, dppf<XOR1>(M));
    const float w0 = __builtin_amdgcn_exp2f(m0 - M), w1 = __builtin_amdgcn_exp2f(m1 - M);
    float Ls = w0 + w1;
    Ls += dppf<XOR1>(Ls);
    const float inv = __builtin_amdgcn_rcpf(Ls), c0 = w0 * inv, c1 = w1 * inv;
    const unsigned a0[4] = {y0[0] & 0xFFFFFF00u, y0[1] & 0xFFFFFF00u, y0[2] & 0xFFFFFF00u, (y0[0] << 24) | ((y0[1] & 0xFFu) << 16) | ((y0[2] & 0xFFu) << 8)};
    const unsigned a1[4] = {y1[0] & 0xFFFFFF00u, y1[1] & 0xFFFFFF00u, y1[2] & 0xFFFFFF00u, (y1[0] << 24) | ((y1[1] & 0xFFu) << 16) | ((y1[2] & 0xFFu) << 8)};
    float sv[4];
#pragma unroll
    for (int j = 0; j < 4; ++j) sv[j] = fmaf(u2f(a0[j]), c0, u2f(a1[j]) * c1);
    const float t0_ = swap32_add(sv[0], sv[1]), t1_ = swap32_add(sv[2], sv[3]);
    float s = swap16_add(t0_, t1_);
    s += dppf<XOR1>(s); s += dppf<XOR2>(s); s += dppf<HMIR>(s); s += dppf<MIR>(s);
    float o = xl_ + rbf(rbf(s) * g2l);
    if (dead) o = __builtin_nanf("");
    if ((ln & 15) == 0) ((GAS unsigned short*)p.out)[cb * DM + 4 * cg + jo] = (unsigned short)f2bf(o);
  }
}
}

extern "C" void kernel_launch(void* const* d_in, const int* in_sizes, int n_in, void* d_out, int out_size, void* d_ws,
                              size_t ws_size, hipStream_t stream) {
  Params p;
  p.x = (const unsigned short*)d_in[0];
  p.k = (const unsigned short*)d_in[1];
  p.v = (const unsigned short*)d_in[2];
  p.wq = (const unsigned short*)d_in[3];
  p.wo = (const unsigned short*)d_in[4];
  p.wg = (const unsigned short*)d_in[6];
  p.wu = (const unsigned short*)d_in[7];
  p.wd = (const unsigned short*)d_in[8];
  p.out = (unsigned short*)d_out;
  p.ws = (unsigned char*)d_ws;
  hipLaunchKernelGGL(fused_decode_kernel, dim3(NWG), dim3(NTHR), 0, stream, p);
}
```
